```python
import torch

def _softmax(x, axis=-1):
    return torch.softmax(x, dim=axis)
def _gelu(x):
    return torch.nn.functional.gelu(x, approximate="tanh")
def _silu(x):
    return torch.nn.functional.silu(x)
def _sigmoid(x):
    return torch.sigmoid(x)
def _layernorm(x, axis=-1, eps=1e-5):
    m = x.mean(dim=axis, keepdim=True)
    v = x.var(dim=axis, keepdim=True, unbiased=False)
    return (x - m) / torch.sqrt(v + eps)
def _scatter_add(tgt, idx, src, axis=0):
    return tgt.clone().index_add_(axis, idx, src)
def _segment_sum(src, seg, n):
    out = torch.zeros((n,) + src.shape[1:], dtype=src.dtype)
    return out.index_add_(0, seg, src)
def _pad(x, pw):
    flat = tuple(v for pair in reversed(pw) for v in pair)
    return torch.nn.functional.pad(x, flat)

kernel_name = 'concat_heads__mha_block_cached__gqa_block_cached__near_ridge'

def setup_inputs():

    _w = (lambda a: (lambda t: t.to(torch.bfloat16) if t.is_floating_point() else t)(torch.as_tensor(a)))
    def _A_setup():
        B, Sq, D = 8, 192, 512
        H, Sk, Dh = 32, 1024, 128
        return {
            "x": _w((lambda *s: torch.randn(*s, dtype=torch.bfloat16) * 0.8)(B, Sq, D)),
            "k_cache": _w((lambda *s: torch.randn(*s, dtype=torch.bfloat16) * 0.8)(B, H, Sk, Dh)),
            "v_cache": _w((lambda *s: torch.randn(*s, dtype=torch.bfloat16) * 0.05)(B, H, Sk, Dh)),
            "wq": _w((lambda *s: torch.randn(*s, dtype=torch.bfloat16) * 0.8)(D, H * Dh) * (D ** -0.5)),
            "wo": _w((lambda *s: torch.randn(*s, dtype=torch.bfloat16) * 0.05)(H * Dh, D) * ((H * Dh) ** -0.5)),
        }
    _A_in = _A_setup()

    def _B_setup():
        B, Sq, D = 8, 192, 512
        Hq, Hkv, Sk, Dh = 32, 32, 1024, 128
        assert Hq % Hkv == 0, (Hq, Hkv)
        return {
            "x": _w((lambda *s: torch.randn(*s, dtype=torch.bfloat16) * 0.8)(B, Sq, D)),
            "k_cache": _w((lambda *s: torch.randn(*s, dtype=torch.bfloat16) * 0.8)(B, Hkv, Sk, Dh)),
            "v_cache": _w((lambda *s: torch.randn(*s, dtype=torch.bfloat16) * 0.05)(B, Hkv, Sk, Dh)),
            "wq": _w((lambda *s: torch.randn(*s, dtype=torch.bfloat16) * 0.8)(D, Hq * Dh) * (D ** -0.5)),
            "wo": _w((lambda *s: torch.randn(*s, dtype=torch.bfloat16) * 0.05)(Hq * Dh, D) * ((Hq * Dh) ** -0.5)),
        }
    _B_in = _B_setup()

    return {
        "A_x": _A_in["x"],
        "A_k_cache": _A_in["k_cache"],
        "A_v_cache": _A_in["v_cache"],
        "A_wq": _A_in["wq"],
        "A_wo": _A_in["wo"],
        "B_x": _B_in["x"],
        "B_k_cache": _B_in["k_cache"],
        "B_v_cache": _B_in["v_cache"],
        "B_wq": _B_in["wq"],
        "B_wo": _B_in["wo"],
    }

def reference(A_x, A_k_cache, A_v_cache, A_wq, A_wo, B_x, B_k_cache, B_v_cache, B_wq, B_wo):
    def _A_ref(x, k_cache, v_cache, wq, wo):
        B, Sq, D = x.shape
        H, Dh = 32, 128
        q = (lambda x, *d: x.permute(*d))((x @ wq).reshape(B, Sq, H, Dh), 0, 2, 1, 3)
        scores = torch.einsum("bhqd,bhkd->bhqk", q, k_cache) * (Dh ** -0.5)
        p = _softmax(scores, axis=-1)
        o = torch.einsum("bhqk,bhkd->bhqd", p, v_cache)
        return (lambda x, *d: x.permute(*d))(o, 0, 2, 1, 3).reshape(B, Sq, H * Dh) @ wo

    def _B_ref(x, k_cache, v_cache, wq, wo):
        B, Sq, D = x.shape
        Hq, Dh = 32, 128
        Hkv = k_cache.shape[1]
        rep = Hq // Hkv
        q = (lambda x, *d: x.permute(*d))((x @ wq).reshape(B, Sq, Hq, Dh), 0, 2, 1, 3)
        k = (lambda x, n, axis: torch.repeat_interleave(x, n, dim=axis))(k_cache, rep, axis=1)
        v = (lambda x, n, axis: torch.repeat_interleave(x, n, dim=axis))(v_cache, rep, axis=1)
        scores = torch.einsum("bhqd,bhkd->bhqk", q, k) * (Dh ** -0.5)
        p = _softmax(scores, axis=-1)
        o = torch.einsum("bhqk,bhkd->bhqd", p, v)
        return (lambda x, *d: x.permute(*d))(o, 0, 2, 1, 3).reshape(B, Sq, Hq * Dh) @ wo

    a = _A_ref(A_x, A_k_cache, A_v_cache, A_wq, A_wo)
    b = _B_ref(B_x, B_k_cache, B_v_cache, B_wq, B_wo)
    return torch.concatenate([a, b], axis=-1)
```

```cpp
#include <hip/hip_runtime.h>
#include <hip/hip_bf16.h>

typedef short bf16x8 __attribute__((ext_vector_type(8)));
typedef short bf16x4 __attribute__((ext_vector_type(4)));
typedef float f32x4 __attribute__((ext_vector_type(4)));
typedef unsigned u32x4 __attribute__((ext_vector_type(4)));
typedef unsigned u32x2 __attribute__((ext_vector_type(2)));
typedef __attribute__((address_space(3))) bf16x4 lds_bf16x4;

#define LDS_PTR(T, off) ((__attribute__((address_space(3))) T*)(off))

constexpr int NB = 8, SQ = 192, DM = 512, NH = 32, SK = 1024, DH = 128;
constexpr int MROWS = NB * SQ;
constexpr int HD = NH * DH;

typedef __bf16 bf16x2_t __attribute__((ext_vector_type(2)));
typedef float f32x2 __attribute__((ext_vector_type(2)));
__device__ __forceinline__ unsigned cvtpk(float lo, float hi) {
  f32x2 v = {lo, hi};
  bf16x2_t r = __builtin_convertvector(v, bf16x2_t);
  return __builtin_bit_cast(unsigned, r);
}

template <int MODE>
__global__ __launch_bounds__(256, 2) void gemm_kernel(const __hip_bfloat16* __restrict__ A0, const __hip_bfloat16* __restrict__ A1,
                                                      const __hip_bfloat16* __restrict__ B0, const __hip_bfloat16* __restrict__ B1,
                                                      __hip_bfloat16* __restrict__ C) {
  constexpr int N = MODE == 0 ? HD : DM;
  constexpr int K = MODE == 0 ? DM : HD;
  constexpr int NTN = N / 128, NTM = MROWS / 128;
  extern __shared__ __attribute__((aligned(16))) char smem[];
  const int tid = threadIdx.x, lane = tid & 63, wid = __builtin_amdgcn_readfirstlane(tid >> 6);
  const int bid = blockIdx.x;
  const int br = bid / (NTM * NTN);
  const int t = bid % (NTM * NTN);
  const int tm = t % NTM, tn = t / NTM;
  const __hip_bfloat16* A = (br ? A1 : A0) + (size_t)tm * 128 * K;
  const __hip_bfloat16* B = (br ? B1 : B0) + (size_t)tn * 128;
  const int wm = wid >> 1, wn = wid & 1;
  const int fr = lane & 15, fg = lane >> 4;

  auto stage = [&](int buf, int kt) {
    char* sA = smem + buf * 32768;
    char* sB = sA + 16384;
#pragma unroll
    for (int i = 0; i < 4; ++i) {
      int lin = (wid * 4 + i) * 1024 + lane * 16;
      int row = lin >> 7, pos = (lin >> 4) & 7;
      int ch = pos ^ (row & 7);
      __builtin_amdgcn_global_load_lds((const unsigned*)(A + (size_t)row * K + kt * 64 + ch * 8), (unsigned*)(sA + lin), 16, 0, 0);
    }
#pragma unroll
    for (int i = 0; i < 4; ++i) {
      int lin = (wid * 4 + i) * 1024 + lane * 16;
      int row = lin >> 8, pos = (lin >> 4) & 15;
      int f = ((row & 3) | ((row >> 1) & 4)) << 1;
      int ch = pos ^ f;
      __builtin_amdgcn_global_load_lds((const unsigned*)(B + (size_t)(kt * 64 + row) * N + ch * 8), (unsigned*)(sB + lin), 16, 0, 0);
    }
  };

  f32x4 acc[4][4];
#pragma unroll
  for (int i = 0; i < 4; ++i)
#pragma unroll
    for (int j = 0; j < 4; ++j) acc[i][j] = f32x4{0.f, 0.f, 0.f, 0.f};

  constexpr int NK = K / 64;
  stage(0, 0);
  for (int kt = 0; kt < NK; ++kt) {
    asm volatile("s_waitcnt vmcnt(0)" ::: "memory");
    __builtin_amdgcn_s_barrier();
    if (kt + 1 < NK) stage((kt + 1) & 1, kt + 1);
    const char* sA = smem + (kt & 1) * 32768;
    const char* sB = sA + 16384;
#pragma unroll
    for (int ks = 0; ks < 2; ++ks) {
      bf16x8 af[4], bfr[4];
#pragma unroll
      for (int i = 0; i < 4; ++i) {
        int row = wm * 64 + i * 16 + fr;
        int ch = 4 * ks + fg;
        af[i] = *(const bf16x8*)(sA + row * 128 + ((ch ^ (row & 7)) << 4));
      }
#pragma unroll
      for (int j = 0; j < 4; ++j) {
        int c = wn * 4 + j;
        int q = fr >> 2, p = fr & 3;
#pragma unroll
        for (int tt = 0; tt < 2; ++tt) {
          int row = 32 * ks + 8 * fg + 4 * tt + q;
          int f = ((row & 3) | ((row >> 1) & 4)) << 1;
          const char* ad = sB + row * 256 + (((2 * c + (p >> 1)) ^ f) << 4) + 8 * (p & 1);
          bf16x4 v = __builtin_amdgcn_ds_read_tr16_b64_v4i16((lds_bf16x4*)ad);
          bfr[j][4 * tt + 0] = v[0]; bfr[j][4 * tt + 1] = v[1]; bfr[j][4 * tt + 2] = v[2]; bfr[j][4 * tt + 3] = v[3];
        }
      }
#pragma unroll
      for (int i = 0; i < 4; ++i)
#pragma unroll
        for (int j = 0; j < 4; ++j)
          acc[i][j] = __builtin_amdgcn_mfma_f32_16x16x32_bf16(bfr[j], af[i], acc[i][j], 0, 0, 0);
    }
  }
#pragma unroll
  for (int i = 0; i < 4; ++i) {
    int m = tm * 128 + wm * 64 + i * 16 + fr;
#pragma unroll
    for (int j = 0; j < 4; ++j) {
      int n = tn * 128 + wn * 64 + j * 16 + 4 * fg;
      u32x2 w = {cvtpk(acc[i][j][0], acc[i][j][1]), cvtpk(acc[i][j][2], acc[i][j][3])};
      size_t idx;
      if (MODE == 0) {
        int b = m / SQ, s = m % SQ, h = n >> 7, d = n & 127;
        idx = ((size_t)((br * NB + b) * NH + h) * SQ + s) * DH + d;
      } else {
        idx = (size_t)m * 1024 + br * 512 + n;
      }
      *(u32x2*)(C + idx) = w;
    }
  }
}

constexpr int KT = 32, NSTG = 4, STG_BYTES = KT * DH * 2 * 2, NIT = SK / KT;

struct AttnState {
  f32x4 oacc[8][3];
  float m[3], l[3];
};

__global__ __launch_bounds__(256, 2) void attn_kernel(const __hip_bfloat16* __restrict__ KA, const __hip_bfloat16* __restrict__ VA,
                                                      const __hip_bfloat16* __restrict__ KB, const __hip_bfloat16* __restrict__ VB,
                                                      const __hip_bfloat16* __restrict__ Q, __hip_bfloat16* __restrict__ O) {
  extern __shared__ __attribute__((aligned(16))) char smem[];
  const int tid = threadIdx.x, lane = tid & 63, wid = __builtin_amdgcn_readfirstlane(tid >> 6);
  const int bid = blockIdx.x;
  const int br = bid >> 8, bh = bid & 255, b = bh >> 5, h = bh & 31;
  const __hip_bfloat16* Kp = (br ? KB : KA) + (size_t)bh * SK * DH;
  const __hip_bfloat16* Vp = (br ? VB : VA) + (size_t)bh * SK * DH;
  const __hip_bfloat16* Qp = Q + (size_t)bid * SQ * DH;
  const int fr = lane & 15, fg = lane >> 4;

  auto stage = [&](int slot, int it) {
    char* sK = smem + slot * STG_BYTES;
    char* sV = sK + KT * DH * 2;
#pragma unroll
    for (int i = 0; i < 2; ++i) {
      int lin = (wid * 2 + i) * 1024 + lane * 16;
      int row = lin >> 8, pos = (lin >> 4) & 15;
      __builtin_amdgcn_global_load_lds((const unsigned*)(Kp + (size_t)(it * KT + row) * DH + ((pos ^ (row & 15)) << 3)), (unsigned*)(sK + lin), 16, 0, 0);
      __builtin_amdgcn_global_load_lds((const unsigned*)(Vp + (size_t)(it * KT + row) * DH + ((pos ^ ((row & 7) << 1)) << 3)), (unsigned*)(sV + lin), 16, 0, 0);
    }
  };

  stage(0, 0); stage(1, 1); stage(2, 2);

  bf16x8 qf[3][4];
#pragma unroll
  for (int j = 0; j < 3; ++j)
#pragma unroll
    for (int ks = 0; ks < 4; ++ks)
      qf[j][ks] = *(const bf16x8*)(Qp + (size_t)(wid * 48 + j * 16 + fr) * DH + ks * 32 + fg * 8);

  f32x4 oacc[8][3];
  float m[3], l[3];
#pragma unroll
  for (int j = 0; j < 3; ++j) {
    m[j] = -__builtin_inff(); l[j] = 0.f;
#pragma unroll
    for (int c = 0; c < 8; ++c) oacc[c][j] = f32x4{0.f, 0.f, 0.f, 0.f};
  }
  const float cs = 0.08838834764831845f * 1.4426950408889634f;

  for (int it = 0; it < NIT; ++it) {
    if (it < NIT - 2) asm volatile("s_waitcnt vmcnt(8)" ::: "memory");
    else if (it == NIT - 2) asm volatile("s_waitcnt vmcnt(4)" ::: "memory");
    else asm volatile("s_waitcnt vmcnt(0)" ::: "memory");
    __builtin_amdgcn_s_barrier();
    if (it + 3 < NIT) stage((it + 3) & 3, it + 3);
    const char* sK = smem + (it & 3) * STG_BYTES;
    const char* sV = sK + KT * DH * 2;
    f32x4 s[2][3];
#pragma unroll
    for (int j = 0; j < 3; ++j) { s[0][j] = f32x4{0.f, 0.f, 0.f, 0.f}; s[1][j] = f32x4{0.f, 0.f, 0.f, 0.f}; }
#pragma unroll
    for (int ks = 0; ks < 4; ++ks) {
      bf16x8 kf0 = *(const bf16x8*)(sK + fr * 256 + (((4 * ks + fg) ^ fr) << 4));
      bf16x8 kf1 = *(const bf16x8*)(sK + (16 + fr) * 256 + (((4 * ks + fg) ^ fr) << 4));
#pragma unroll
      for (int j = 0; j < 3; ++j) {
        s[0][j] = __builtin_amdgcn_mfma_f32_16x16x32_bf16(kf0, qf[j][ks], s[0][j], 0, 0, 0);
        s[1][j] = __builtin_amdgcn_mfma_f32_16x16x32_bf16(kf1, qf[j][ks], s[1][j], 0, 0, 0);
      }
    }
    bf16x8 pb[3];
#pragma unroll
    for (int j = 0; j < 3; ++j) {
      float mx = fmaxf(fmaxf(fmaxf(s[0][j][0], s[0][j][1]), fmaxf(s[0][j][2], s[0][j][3])),
                       fmaxf(fmaxf(s[1][j][0], s[1][j][1]), fmaxf(s[1][j][2], s[1][j][3])));
      {
        auto r = __builtin_amdgcn_permlane32_swap(__float_as_uint(mx), __float_as_uint(mx), false, false);
        mx = fmaxf(__uint_as_float(r[0]), __uint_as_float(r[1]));
        auto r2 = __builtin_amdgcn_permlane16_swap(__float_as_uint(mx), __float_as_uint(mx), false, false);
        mx = fmaxf(__uint_as_float(r2[0]), __uint_as_float(r2[1]));
      }
      float mn = fmaxf(m[j], mx);
      float alpha = __builtin_amdgcn_exp2f((m[j] - mn) * cs);
      m[j] = mn;
      float nm = -mn * cs;
      float p[8];
#pragma unroll
      for (int r = 0; r < 4; ++r) {
        p[r] = __builtin_amdgcn_exp2f(fmaf(s[0][j][r], cs, nm));
        p[4 + r] = __builtin_amdgcn_exp2f(fmaf(s[1][j][r], cs, nm));
      }
      float ps = ((p[0] + p[1]) + (p[2] + p[3])) + ((p[4] + p[5]) + (p[6] + p[7]));
      l[j] = l[j] * alpha + ps;
#pragma unroll
      for (int c = 0; c < 8; ++c) oacc[c][j] *= alpha;
      u32x4 w = {cvtpk(p[0], p[1]), cvtpk(p[2], p[3]), cvtpk(p[4], p[5]), cvtpk(p[6], p[7])};
      pb[j] = *(bf16x8*)&w;
    }
#pragma unroll
    for (int c = 0; c < 8; ++c) {
      bf16x8 vf;
      int q4 = fr >> 2, p4 = fr & 3;
#pragma unroll
      for (int tt = 0; tt < 2; ++tt) {
        int row = 16 * tt + 4 * fg + q4;
        const char* ad = sV + row * 256 + (((2 * c + (p4 >> 1)) ^ ((row & 7) << 1)) << 4) + 8 * (p4 & 1);
        bf16x4 v = __builtin_amdgcn_ds_read_tr16_b64_v4i16((lds_bf16x4*)ad);
        vf[4 * tt + 0] = v[0]; vf[4 * tt + 1] = v[1]; vf[4 * tt + 2] = v[2]; vf[4 * tt + 3] = v[3];
      }
#pragma unroll
      for (int j = 0; j < 3; ++j) oacc[c][j] = __builtin_amdgcn_mfma_f32_16x16x32_bf16(vf, pb[j], oacc[c][j], 0, 0, 0);
    }
  }
#pragma unroll
  for (int j = 0; j < 3; ++j) {
    float ls = l[j];
    auto r = __builtin_amdgcn_permlane32_swap(__float_as_uint(ls), __float_as_uint(ls), false, false);
    ls = __uint_as_float(r[0]) + __uint_as_float(r[1]);
    auto r2 = __builtin_amdgcn_permlane16_swap(__float_as_uint(ls), __float_as_uint(ls), false, false);
    ls = __uint_as_float(r2[0]) + __uint_as_float(r2[1]);
    float inv = 1.f / ls;
    int qrow = b * SQ + wid * 48 + j * 16 + fr;
    __hip_bfloat16* op = O + (size_t)br * MROWS * HD + (size_t)qrow * HD + h * DH + 4 * fg;
#pragma unroll
    for (int c = 0; c < 8; ++c) {
      u32x2 w = {cvtpk(oacc[c][j][0] * inv, oacc[c][j][1] * inv), cvtpk(oacc[c][j][2] * inv, oacc[c][j][3] * inv)};
      *(u32x2*)(op + c * 16) = w;
    }
  }
}

extern "C" void kernel_launch(void* const* d_in, const int* in_sizes, int n_in, void* d_out, int out_size, void* d_ws, size_t ws_size,
                              hipStream_t stream) {
  const __hip_bfloat16* A_x = (const __hip_bfloat16*)d_in[0];
  const __hip_bfloat16* A_k = (const __hip_bfloat16*)d_in[1];
  const __hip_bfloat16* A_v = (const __hip_bfloat16*)d_in[2];
  const __hip_bfloat16* A_wq = (const __hip_bfloat16*)d_in[3];
  const __hip_bfloat16* A_wo = (const __hip_bfloat16*)d_in[4];
  const __hip_bfloat16* B_x = (const __hip_bfloat16*)d_in[5];
  const __hip_bfloat16* B_k = (const __hip_bfloat16*)d_in[6];
  const __hip_bfloat16* B_v = (const __hip_bfloat16*)d_in[7];
  const __hip_bfloat16* B_wq = (const __hip_bfloat16*)d_in[8];
  const __hip_bfloat16* B_wo = (const __hip_bfloat16*)d_in[9];
  __hip_bfloat16* q = (__hip_bfloat16*)d_ws;
  __hip_bfloat16* o = q + (size_t)2 * MROWS * HD;
  __hip_bfloat16* out = (__hip_bfloat16*)d_out;

  (void)hipFuncSetAttribute((const void*)gemm_kernel<0>, hipFuncAttributeMaxDynamicSharedMemorySize, 65536);
  (void)hipFuncSetAttribute((const void*)gemm_kernel<1>, hipFuncAttributeMaxDynamicSharedMemorySize, 65536);
  (void)hipFuncSetAttribute((const void*)attn_kernel, hipFuncAttributeMaxDynamicSharedMemorySize, 65536);
  gemm_kernel<0><<<2 * 12 * 32, 256, 65536, stream>>>(A_x, B_x, A_wq, B_wq, q);
  attn_kernel<<<512, 256, NSTG * STG_BYTES, stream>>>(A_k, A_v, B_k, B_v, q, o);
  gemm_kernel<1><<<2 * 12 * 4, 256, 65536, stream>>>(o, o + (size_t)MROWS * HD, A_wo, B_wo, out);
}
```

```cpp
#include <hip/hip_runtime.h>
#include <hip/hip_bf16.h>

typedef short bf16x8 __attribute__((ext_vector_type(8)));
typedef short bf16x4 __attribute__((ext_vector_type(4)));
typedef float f32x4 __attribute__((ext_vector_type(4)));
typedef unsigned u32x4 __attribute__((ext_vector_type(4)));
typedef unsigned u32x2 __attribute__((ext_vector_type(2)));
typedef __attribute__((address_space(3))) bf16x4 lds_bf16x4;

#define LDS_PTR(T, off) ((__attribute__((address_space(3))) T*)(off))

constexpr int NB = 8, SQ = 192, DM = 512, NH = 32, SK = 1024, DH = 128;
constexpr int MROWS = NB * SQ;
constexpr int HD = NH * DH;

typedef __bf16 bf16x2_t __attribute__((ext_vector_type(2)));
typedef float f32x2 __attribute__((ext_vector_type(2)));
__device__ __forceinline__ unsigned cvtpk(float lo, float hi) {
  f32x2 v = {lo, hi};
  bf16x2_t r = __builtin_convertvector(v, bf16x2_t);
  return __builtin_bit_cast(unsigned, r);
}

template <int MODE>
__global__ __launch_bounds__(256, 2) void gemm_kernel(const __hip_bfloat16* __restrict__ A0, const __hip_bfloat16* __restrict__ A1,
                                                      const __hip_bfloat16* __restrict__ B0, const __hip_bfloat16* __restrict__ B1,
                                                      __hip_bfloat16* __restrict__ C) {
  constexpr int N = MODE == 0 ? HD : DM;
  constexpr int K = MODE == 0 ? DM : HD;
  constexpr int NTN = N / 128, NTM = MROWS / 128;
  extern __shared__ __attribute__((aligned(16))) char smem[];
  const int tid = threadIdx.x, lane = tid & 63, wid = __builtin_amdgcn_readfirstlane(tid >> 6);
  const int bid = blockIdx.x;
  const int br = bid / (NTM * NTN);
  const int t = bid % (NTM * NTN);
  const int tm = t % NTM, tn = t / NTM;
  const __hip_bfloat16* A = (br ? A1 : A0) + (size_t)tm * 128 * K;
  const __hip_bfloat16* B = (br ? B1 : B0) + (size_t)tn * 128;
  const int wm = wid >> 1, wn = wid & 1;
  const int fr = lane & 15, fg = lane >> 4;

  auto stage = [&](int buf, int kt) {
    char* sA = smem + buf * 32768;
    char* sB = sA + 16384;
#pragma unroll
    for (int i = 0; i < 4; ++i) {
      int lin = (wid * 4 + i) * 1024 + lane * 16;
      int row = lin >> 7, pos = (lin >> 4) & 7;
      int ch = pos ^ (row & 7);
      __builtin_amdgcn_global_load_lds((const unsigned*)(A + (size_t)row * K + kt * 64 + ch * 8), (unsigned*)(sA + lin), 16, 0, 0);
    }
#pragma unroll
    for (int i = 0; i < 4; ++i) {
      int lin = (wid * 4 + i) * 1024 + lane * 16;
      int row = lin >> 8, pos = (lin >> 4) & 15;
      int f = ((row & 3) | ((row >> 1) & 4)) << 1;
      int ch = pos ^ f;
      __builtin_amdgcn_global_load_lds((const unsigned*)(B + (size_t)(kt * 64 + row) * N + ch * 8), (unsigned*)(sB + lin), 16, 0, 0);
    }
  };

  f32x4 acc[4][4];
#pragma unroll
  for (int i = 0; i < 4; ++i)
#pragma unroll
    for (int j = 0; j < 4; ++j) acc[i][j] = f32x4{0.f, 0.f, 0.f, 0.f};

  constexpr int NK = K / 64;
  stage(0, 0);
  for (int kt = 0; kt < NK; ++kt) {
    asm volatile("s_waitcnt vmcnt(0)" ::: "memory");
    __builtin_amdgcn_s_barrier();
    if (kt + 1 < NK) stage((kt + 1) & 1, kt + 1);
    const char* sA = smem + (kt & 1) * 32768;
    const char* sB = sA + 16384;
#pragma unroll
    for (int ks = 0; ks < 2; ++ks) {
      bf16x8 af[4], bfr[4];
#pragma unroll
      for (int i = 0; i < 4; ++i) {
        int row = wm * 64 + i * 16 + fr;
        int ch = 4 * ks + fg;
        af[i] = *(const bf16x8*)(sA + row * 128 + ((ch ^ (row & 7)) << 4));
      }
#pragma unroll
      for (int j = 0; j < 4; ++j) {
        int c = wn * 4 + j;
        int q = fr >> 2, p = fr & 3;
#pragma unroll
        for (int tt = 0; tt < 2; ++tt) {
          int row = 32 * ks + 8 * fg + 4 * tt + q;
          int f = ((row & 3) | ((row >> 1) & 4)) << 1;
          const char* ad = sB + row * 256 + (((2 * c + (p >> 1)) ^ f) << 4) + 8 * (p & 1);
          bf16x4 v = __builtin_amdgcn_ds_read_tr16_b64_v4i16((lds_bf16x4*)ad);
          bfr[j][4 * tt + 0] = v[0]; bfr[j][4 * tt + 1] = v[1]; bfr[j][4 * tt + 2] = v[2]; bfr[j][4 * tt + 3] = v[3];
        }
      }
#pragma unroll
      for (int i = 0; i < 4; ++i)
#pragma unroll
        for (int j = 0; j < 4; ++j)
          acc[i][j] = __builtin_amdgcn_mfma_f32_16x16x32_bf16(bfr[j], af[i], acc[i][j], 0, 0, 0);
    }
  }
#pragma unroll
  for (int i = 0; i < 4; ++i) {
    int m = tm * 128 + wm * 64 + i * 16 + fr;
#pragma unroll
    for (int j = 0; j < 4; ++j) {
      int n = tn * 128 + wn * 64 + j * 16 + 4 * fg;
      u32x2 w = {cvtpk(acc[i][j][0], acc[i][j][1]), cvtpk(acc[i][j][2], acc[i][j][3])};
      size_t idx;
      if (MODE == 0) {
        int b = m / SQ, s = m % SQ, h = n >> 7, d = n & 127;
        idx = ((size_t)((br * NB + b) * NH + h) * SQ + s) * DH + d;
      } else {
        idx = (size_t)m * 1024 + br * 512 + n;
      }
      *(u32x2*)(C + idx) = w;
    }
  }
}

constexpr int KT = 32, NSTG = 4, STG_BYTES = KT * DH * 2 * 2, NIT = SK / KT;

struct AttnState {
  f32x4 oacc[8][3];
  float m[3], l[3];
};

__global__ __launch_bounds__(256, 2) void attn_kernel(const __hip_bfloat16* __restrict__ KA, const __hip_bfloat16* __restrict__ VA,
                                                      const __hip_bfloat16* __restrict__ KB, const __hip_bfloat16* __restrict__ VB,
                                                      const __hip_bfloat16* __restrict__ Q, __hip_bfloat16* __restrict__ O) {
  extern __shared__ __attribute__((aligned(16))) char smem[];
  const int tid = threadIdx.x, lane = tid & 63, wid = __builtin_amdgcn_readfirstlane(tid >> 6);
  const int bid = blockIdx.x;
  const int br = bid >> 8, bh = bid & 255, b = bh >> 5, h = bh & 31;
  const __hip_bfloat16* Kp = (br ? KB : KA) + (size_t)bh * SK * DH;
  const __hip_bfloat16* Vp = (br ? VB : VA) + (size_t)bh * SK * DH;
  const __hip_bfloat16* Qp = Q + (size_t)bid * SQ * DH;
  const int fr = lane & 15, fg = lane >> 4;

  auto stage = [&](int slot, int it) {
    char* sK = smem + slot * STG_BYTES;
    char* sV = sK + KT * DH * 2;
#pragma unroll
    for (int i = 0; i < 2; ++i) {
      int lin = (wid * 2 + i) * 1024 + lane * 16;
      int row = lin >> 8, pos = (lin >> 4) & 15;
      __builtin_amdgcn_global_load_lds((const unsigned*)(Kp + (size_t)(it * KT + row) * DH + ((pos ^ (row & 15)) << 3)), (unsigned*)(sK + lin), 16, 0, 0);
      __builtin_amdgcn_global_load_lds((const unsigned*)(Vp + (size_t)(it * KT + row) * DH + ((pos ^ ((row & 7) << 1)) << 3)), (unsigned*)(sV + lin), 16, 0, 0);
    }
  };

  stage(0, 0); stage(1, 1); stage(2, 2);

  bf16x8 qf[3][4];
#pragma unroll
  for (int j = 0; j < 3; ++j)
#pragma unroll
    for (int ks = 0; ks < 4; ++ks)
      qf[j][ks] = *(const bf16x8*)(Qp + (size_t)(wid * 48 + j * 16 + fr) * DH + ks * 32 + fg * 8);

  f32x4 oacc[8][3];
  float m[3], l[3];
#pragma unroll
  for (int j = 0; j < 3; ++j) {
    m[j] = -__builtin_inff(); l[j] = 0.f;
#pragma unroll
    for (int c = 0; c < 8; ++c) oacc[c][j] = f32x4{0.f, 0.f, 0.f, 0.f};
  }
  const float cs = 0.08838834764831845f * 1.4426950408889634f;

  unsigned kaddr[4], vaddr[8];
#pragma unroll
  for (int ks = 0; ks < 4; ++ks) kaddr[ks] = (unsigned)(fr * 256 + (((4 * ks + fg) ^ fr) << 4));
  {
    int q4 = fr >> 2, p4 = fr & 3, row = 4 * fg + q4;
#pragma unroll
    for (int c = 0; c < 8; ++c) vaddr[c] = (unsigned)(KT * DH * 2 + row * 256 + (((2 * c + (p4 >> 1)) ^ ((row & 7) << 1)) << 4) + 8 * (p4 & 1));
  }

  for (int it = 0; it < NIT; ++it) {
    if (it < NIT - 2) asm volatile("s_waitcnt vmcnt(8)" ::: "memory");
    else if (it == NIT - 2) asm volatile("s_waitcnt vmcnt(4)" ::: "memory");
    else asm volatile("s_waitcnt vmcnt(0)" ::: "memory");
    __builtin_amdgcn_s_barrier();
    if (it + 3 < NIT) stage((it + 3) & 3, it + 3);
    const char* sK = smem + (it & 3) * STG_BYTES;
    const char* sV = sK + KT * DH * 2;
    const unsigned kb = (unsigned)(size_t)sK;
    bf16x8 kf[2][4];
#pragma unroll
    for (int ks = 0; ks < 4; ++ks) {
      unsigned a = kaddr[ks] + (unsigned)((it & 3) * STG_BYTES);
      asm volatile("ds_read_b128 %0, %1" : "=v"(kf[0][ks]) : "v"(a) : "memory");
      asm volatile("ds_read_b128 %0, %1 offset:4096" : "=v"(kf[1][ks]) : "v"(a) : "memory");
    }
    asm volatile("s_waitcnt lgkmcnt(0)" ::: "memory");
    __builtin_amdgcn_sched_barrier(0);
    f32x4 s[2][3];
#pragma unroll
    for (int j = 0; j < 3; ++j) { s[0][j] = f32x4{0.f, 0.f, 0.f, 0.f}; s[1][j] = f32x4{0.f, 0.f, 0.f, 0.f}; }
#pragma unroll
    for (int ks = 0; ks < 4; ++ks) {
#pragma unroll
      for (int j = 0; j < 3; ++j) {
        s[0][j] = __builtin_amdgcn_mfma_f32_16x16x32_bf16(kf[0][ks], qf[j][ks], s[0][j], 0, 0, 0);
        s[1][j] = __builtin_amdgcn_mfma_f32_16x16x32_bf16(kf[1][ks], qf[j][ks], s[1][j], 0, 0, 0);
      }
    }
    __builtin_amdgcn_sched_barrier(0);
    bf16x4 vlo[8], vhi[8];
#pragma unroll
    for (int c = 0; c < 8; ++c) {
      unsigned a = vaddr[c >> 1] + (unsigned)((it & 3) * STG_BYTES);
      a = vaddr[c] + (unsigned)((it & 3) * STG_BYTES);
      asm volatile("ds_read_b64_tr_b16 %0, %1" : "=v"(vlo[c]) : "v"(a) : "memory");
      asm volatile("ds_read_b64_tr_b16 %0, %1 offset:4096" : "=v"(vhi[c]) : "v"(a) : "memory");
    }
    __builtin_amdgcn_sched_barrier(0);
    bf16x8 pb[3];
#pragma unroll
    for (int j = 0; j < 3; ++j) {
      float mx = fmaxf(fmaxf(fmaxf(s[0][j][0], s[0][j][1]), fmaxf(s[0][j][2], s[0][j][3])),
                       fmaxf(fmaxf(s[1][j][0], s[1][j][1]), fmaxf(s[1][j][2], s[1][j][3])));
      {
        auto r = __builtin_amdgcn_permlane32_swap(__float_as_uint(mx), __float_as_uint(mx), false, false);
        mx = fmaxf(__uint_as_float(r[0]), __uint_as_float(r[1]));
        auto r2 = __builtin_amdgcn_permlane16_swap(__float_as_uint(mx), __float_as_uint(mx), false, false);
        mx = fmaxf(__uint_as_float(r2[0]), __uint_as_float(r2[1]));
      }
      float mn = fmaxf(m[j], mx);
      float alpha = __builtin_amdgcn_exp2f((m[j] - mn) * cs);
      m[j] = mn;
      float nm = -mn * cs;
      float p[8];
#pragma unroll
      for (int r = 0; r < 4; ++r) {
        p[r] = __builtin_amdgcn_exp2f(fmaf(s[0][j][r], cs, nm));
        p[4 + r] = __builtin_amdgcn_exp2f(fmaf(s[1][j][r], cs, nm));
      }
      float ps = ((p[0] + p[1]) + (p[2] + p[3])) + ((p[4] + p[5]) + (p[6] + p[7]));
      l[j] = l[j] * alpha + ps;
#pragma unroll
      for (int c = 0; c < 8; ++c) oacc[c][j] *= alpha;
      u32x4 w = {cvtpk(p[0], p[1]), cvtpk(p[2], p[3]), cvtpk(p[4], p[5]), cvtpk(p[6], p[7])};
      pb[j] = *(bf16x8*)&w;
    }
    asm volatile("s_waitcnt lgkmcnt(0)" ::: "memory");
    __builtin_amdgcn_sched_barrier(0);
#pragma unroll
    for (int c = 0; c < 8; ++c) {
      bf16x8 vf = {vlo[c][0], vlo[c][1], vlo[c][2], vlo[c][3], vhi[c][0], vhi[c][1], vhi[c][2], vhi[c][3]};
#pragma unroll
      for (int j = 0; j < 3; ++j) oacc[c][j] = __builtin_amdgcn_mfma_f32_16x16x32_bf16(vf, pb[j], oacc[c][j], 0, 0, 0);
    }
  }
#pragma unroll
  for (int j = 0; j < 3; ++j) {
    float ls = l[j];
    auto r = __builtin_amdgcn_permlane32_swap(__float_as_uint(ls), __float_as_uint(ls), false, false);
    ls = __uint_as_float(r[0]) + __uint_as_float(r[1]);
    auto r2 = __builtin_amdgcn_permlane16_swap(__float_as_uint(ls), __float_as_uint(ls), false, false);
    ls = __uint_as_float(r2[0]) + __uint_as_float(r2[1]);
    float inv = 1.f / ls;
    int qrow = b * SQ + wid * 48 + j * 16 + fr;
    __hip_bfloat16* op = O + (size_t)br * MROWS * HD + (size_t)qrow * HD + h * DH + 4 * fg;
#pragma unroll
    for (int c = 0; c < 8; ++c) {
      u32x2 w = {cvtpk(oacc[c][j][0] * inv, oacc[c][j][1] * inv), cvtpk(oacc[c][j][2] * inv, oacc[c][j][3] * inv)};
      *(u32x2*)(op + c * 16) = w;
    }
  }
}

extern "C" void kernel_launch(void* const* d_in, const int* in_sizes, int n_in, void* d_out, int out_size, void* d_ws, size_t ws_size,
                              hipStream_t stream) {
  const __hip_bfloat16* A_x = (const __hip_bfloat16*)d_in[0];
  const __hip_bfloat16* A_k = (const __hip_bfloat16*)d_in[1];
  const __hip_bfloat16* A_v = (const __hip_bfloat16*)d_in[2];
  const __hip_bfloat16* A_wq = (const __hip_bfloat16*)d_in[3];
  const __hip_bfloat16* A_wo = (const __hip_bfloat16*)d_in[4];
  const __hip_bfloat16* B_x = (const __hip_bfloat16*)d_in[5];
  const __hip_bfloat16* B_k = (const __hip_bfloat16*)d_in[6];
  const __hip_bfloat16* B_v = (const __hip_bfloat16*)d_in[7];
  const __hip_bfloat16* B_wq = (const __hip_bfloat16*)d_in[8];
  const __hip_bfloat16* B_wo = (const __hip_bfloat16*)d_in[9];
  __hip_bfloat16* q = (__hip_bfloat16*)d_ws;
  __hip_bfloat16* o = q + (size_t)2 * MROWS * HD;
  __hip_bfloat16* out = (__hip_bfloat16*)d_out;

  (void)hipFuncSetAttribute((const void*)gemm_kernel<0>, hipFuncAttributeMaxDynamicSharedMemorySize, 65536);
  (void)hipFuncSetAttribute((const void*)gemm_kernel<1>, hipFuncAttributeMaxDynamicSharedMemorySize, 65536);
  (void)hipFuncSetAttribute((const void*)attn_kernel, hipFuncAttributeMaxDynamicSharedMemorySize, 65536);
  gemm_kernel<0><<<2 * 12 * 32, 256, 65536, stream>>>(A_x, B_x, A_wq, B_wq, q);
  attn_kernel<<<512, 256, NSTG * STG_BYTES, stream>>>(A_k, A_v, B_k, B_v, q, o);
  gemm_kernel<1><<<2 * 12 * 4, 256, 65536, stream>>>(o, o + (size_t)MROWS * HD, A_wo, B_wo, out);
}
```

```cpp
#include <hip/hip_runtime.h>
#include <hip/hip_bf16.h>

typedef short bf16x8 __attribute__((ext_vector_type(8)));
typedef short bf16x4 __attribute__((ext_vector_type(4)));
typedef float f32x4 __attribute__((ext_vector_type(4)));
typedef unsigned u32x4 __attribute__((ext_vector_type(4)));
typedef unsigned u32x2 __attribute__((ext_vector_type(2)));
typedef __attribute__((address_space(3))) bf16x4 lds_bf16x4;

#define LDS_PTR(T, off) ((__attribute__((address_space(3))) T*)(off))

constexpr int NB = 8, SQ = 192, DM = 512, NH = 32, SK = 1024, DH = 128;
constexpr int MROWS = NB * SQ;
constexpr int HD = NH * DH;

typedef __bf16 bf16x2_t __attribute__((ext_vector_type(2)));
typedef float f32x2 __attribute__((ext_vector_type(2)));
__device__ __forceinline__ unsigned cvtpk(float lo, float hi) {
  f32x2 v = {lo, hi};
  bf16x2_t r = __builtin_convertvector(v, bf16x2_t);
  return __builtin_bit_cast(unsigned, r);
}

constexpr int OSPLIT = 4;
template <int MODE>
__global__ __launch_bounds__(256, 2) void gemm_kernel(const __hip_bfloat16* __restrict__ A0, const __hip_bfloat16* __restrict__ A1,
                                                      const __hip_bfloat16* __restrict__ B0, const __hip_bfloat16* __restrict__ B1,
                                                      __hip_bfloat16* __restrict__ C, float* __restrict__ P) {
  constexpr int N = MODE == 0 ? HD : DM;
  constexpr int K = MODE == 0 ? DM : HD;
  constexpr int SPLIT = MODE == 0 ? 1 : OSPLIT;
  constexpr int KS = K / SPLIT;
  constexpr int NTN = N / 128, NTM = MROWS / 128;
  extern __shared__ __attribute__((aligned(16))) char smem[];
  const int tid = threadIdx.x, lane = tid & 63, wid = __builtin_amdgcn_readfirstlane(tid >> 6);
  const int bid = blockIdx.x;
  const int sp = bid % SPLIT;
  const int t0 = bid / SPLIT;
  const int br = t0 / (NTM * NTN);
  const int t = t0 % (NTM * NTN);
  const int tm = t % NTM, tn = t / NTM;
  const __hip_bfloat16* A = (br ? A1 : A0) + (size_t)tm * 128 * K + sp * KS;
  const __hip_bfloat16* B = (br ? B1 : B0) + (size_t)sp * KS * N + (size_t)tn * 128;
  const int wm = wid >> 1, wn = wid & 1;
  const int fr = lane & 15, fg = lane >> 4;

  auto stage = [&](int buf, int kt) {
    char* sA = smem + buf * 32768;
    char* sB = sA + 16384;
#pragma unroll
    for (int i = 0; i < 4; ++i) {
      int lin = (wid * 4 + i) * 1024 + lane * 16;
      int row = lin >> 7, pos = (lin >> 4) & 7;
      int ch = pos ^ (row & 7);
      __builtin_amdgcn_global_load_lds((const unsigned*)(A + (size_t)row * K + kt * 64 + ch * 8), (unsigned*)(sA + lin), 16, 0, 0);
    }
#pragma unroll
    for (int i = 0; i < 4; ++i) {
      int lin = (wid * 4 + i) * 1024 + lane * 16;
      int row = lin >> 8, pos = (lin >> 4) & 15;
      int f = ((row & 3) | ((row >> 1) & 4)) << 1;
      int ch = pos ^ f;
      __builtin_amdgcn_global_load_lds((const unsigned*)(B + (size_t)(kt * 64 + row) * N + ch * 8), (unsigned*)(sB + lin), 16, 0, 0);
    }
  };

  unsigned aaddr[2], baddr[4];
#pragma unroll
  for (int ks = 0; ks < 2; ++ks) aaddr[ks] = (unsigned)((wm * 64 + fr) * 128 + (((4 * ks + fg) ^ (fr & 7)) << 4));
  {
    int q = fr >> 2, p = fr & 3, row = 8 * fg + q;
    int f = ((row & 3) | ((row >> 1) & 4)) << 1;
#pragma unroll
    for (int j = 0; j < 4; ++j) baddr[j] = (unsigned)(16384 + row * 256 + (((2 * (wn * 4 + j) + (p >> 1)) ^ f) << 4) + 8 * (p & 1));
  }

  f32x4 acc[4][4];
#pragma unroll
  for (int i = 0; i < 4; ++i)
#pragma unroll
    for (int j = 0; j < 4; ++j) acc[i][j] = f32x4{0.f, 0.f, 0.f, 0.f};

  constexpr int NK = KS / 64;
  stage(0, 0);
  stage(1, 1);
  int buf = 0;
  for (int kt = 0; kt < NK; ++kt) {
    if (kt + 1 < NK) asm volatile("s_waitcnt vmcnt(8)" ::: "memory");
    else asm volatile("s_waitcnt vmcnt(0)" ::: "memory");
    __builtin_amdgcn_s_barrier();
    if (kt + 2 < NK) stage(buf >= 1 ? buf - 1 : 2, kt + 2);
    const unsigned bo = (unsigned)(buf * 32768);
    bf16x8 af[2][4];
    bf16x4 blo[2][4], bhi[2][4];
#pragma unroll
    for (int ks = 0; ks < 2; ++ks) {
#pragma unroll
      for (int i = 0; i < 4; ++i)
        asm volatile("ds_read_b128 %0, %1 offset:%2" : "=v"(af[ks][i]) : "v"(aaddr[ks] + bo), "i"(i * 2048) : "memory");
#pragma unroll
      for (int j = 0; j < 4; ++j) {
        asm volatile("ds_read_b64_tr_b16 %0, %1 offset:%2" : "=v"(blo[ks][j]) : "v"(baddr[j] + bo), "i"(ks * 8192) : "memory");
        asm volatile("ds_read_b64_tr_b16 %0, %1 offset:%2" : "=v"(bhi[ks][j]) : "v"(baddr[j] + bo), "i"(ks * 8192 + 1024) : "memory");
      }
    }
    asm volatile("s_waitcnt lgkmcnt(0)" ::: "memory");
    __builtin_amdgcn_sched_barrier(0);
#pragma unroll
    for (int ks = 0; ks < 2; ++ks) {
#pragma unroll
      for (int j = 0; j < 4; ++j) {
        bf16x8 bfr = {blo[ks][j][0], blo[ks][j][1], blo[ks][j][2], blo[ks][j][3], bhi[ks][j][0], bhi[ks][j][1], bhi[ks][j][2], bhi[ks][j][3]};
#pragma unroll
        for (int i = 0; i < 4; ++i) acc[i][j] = __builtin_amdgcn_mfma_f32_16x16x32_bf16(bfr, af[ks][i], acc[i][j], 0, 0, 0);
      }
    }
    buf = buf == 2 ? 0 : buf + 1;
  }
#pragma unroll
  for (int i = 0; i < 4; ++i) {
    int m = tm * 128 + wm * 64 + i * 16 + fr;
#pragma unroll
    for (int j = 0; j < 4; ++j) {
      int n = tn * 128 + wn * 64 + j * 16 + 4 * fg;
      if (MODE == 0) {
        u32x2 w = {cvtpk(acc[i][j][0], acc[i][j][1]), cvtpk(acc[i][j][2], acc[i][j][3])};
        int b = m / SQ, s_ = m % SQ, h = n >> 7, d = n & 127;
        size_t idx = ((size_t)((br * NB + b) * NH + h) * SQ + s_) * DH + d;
        *(u32x2*)(C + idx) = w;
      } else {
        *(f32x4*)(P + ((size_t)sp * 2 * MROWS + (size_t)br * MROWS + m) * DM + n) = acc[i][j];
      }
    }
  }
}

__global__ __launch_bounds__(256) void oreduce_kernel(const float* __restrict__ P, __hip_bfloat16* __restrict__ out) {
  int i = blockIdx.x * 256 + threadIdx.x;
  int n4 = i & 127, r = i >> 7;
  f32x4 a = *(const f32x4*)(P + (size_t)r * DM + n4 * 4);
#pragma unroll
  for (int s = 1; s < OSPLIT; ++s) a += *(const f32x4*)(P + ((size_t)s * 2 * MROWS + r) * DM + n4 * 4);
  int br = r / MROWS, m = r % MROWS;
  u32x2 w = {cvtpk(a[0], a[1]), cvtpk(a[2], a[3])};
  *(u32x2*)(out + (size_t)m * 1024 + br * 512 + n4 * 4) = w;
}

constexpr int KT = 32, NSTG = 4, STG_BYTES = KT * DH * 2 * 2, NIT = SK / KT;

struct AttnState {
  f32x4 oacc[8][3];
  float m[3], l[3];
};

__global__ __launch_bounds__(256, 2) void attn_kernel(const __hip_bfloat16* __restrict__ KA, const __hip_bfloat16* __restrict__ VA,
                                                      const __hip_bfloat16* __restrict__ KB, const __hip_bfloat16* __restrict__ VB,
                                                      const __hip_bfloat16* __restrict__ Q, __hip_bfloat16* __restrict__ O) {
  extern __shared__ __attribute__((aligned(16))) char smem[];
  const int tid = threadIdx.x, lane = tid & 63, wid = __builtin_amdgcn_readfirstlane(tid >> 6);
  const int bid = blockIdx.x;
  const int br = bid >> 8, bh = bid & 255, b = bh >> 5, h = bh & 31;
  const __hip_bfloat16* Kp = (br ? KB : KA) + (size_t)bh * SK * DH;
  const __hip_bfloat16* Vp = (br ? VB : VA) + (size_t)bh * SK * DH;
  const __hip_bfloat16* Qp = Q + (size_t)bid * SQ * DH;
  const int fr = lane & 15, fg = lane >> 4;

  auto stage = [&](int slot, int it) {
    char* sK = smem + slot * STG_BYTES;
    char* sV = sK + KT * DH * 2;
#pragma unroll
    for (int i = 0; i < 2; ++i) {
      int lin = (wid * 2 + i) * 1024 + lane * 16;
      int row = lin >> 8, pos = (lin >> 4) & 15;
      __builtin_amdgcn_global_load_lds((const unsigned*)(Kp + (size_t)(it * KT + row) * DH + ((pos ^ (row & 15)) << 3)), (unsigned*)(sK + lin), 16, 0, 0);
      __builtin_amdgcn_global_load_lds((const unsigned*)(Vp + (size_t)(it * KT + row) * DH + ((pos ^ ((row & 7) << 1)) << 3)), (unsigned*)(sV + lin), 16, 0, 0);
    }
  };

  stage(0, 0); stage(1, 1); stage(2, 2);

  bf16x8 qf[3][4];
#pragma unroll
  for (int j = 0; j < 3; ++j)
#pragma unroll
    for (int ks = 0; ks < 4; ++ks)
      qf[j][ks] = *(const bf16x8*)(Qp + (size_t)(wid * 48 + j * 16 + fr) * DH + ks * 32 + fg * 8);

#pragma unroll
  for (int j = 0; j < 3; ++j)
#pragma unroll
    for (int ks = 0; ks < 4; ++ks) asm volatile("" : "+v"(qf[j][ks]));

  f32x4 oacc[8][3];
  float m[3], l[3];
#pragma unroll
  for (int j = 0; j < 3; ++j) {
    m[j] = -__builtin_inff(); l[j] = 0.f;
#pragma unroll
    for (int c = 0; c < 8; ++c) oacc[c][j] = f32x4{0.f, 0.f, 0.f, 0.f};
  }
  const float cs = 0.08838834764831845f * 1.4426950408889634f;
  const float THR = 8.0f;

  unsigned kaddr[4], vaddr[8];
#pragma unroll
  for (int ks = 0; ks < 4; ++ks) kaddr[ks] = (unsigned)(fr * 256 + (((4 * ks + fg) ^ fr) << 4));
  {
    int q4 = fr >> 2, p4 = fr & 3, row = 4 * fg + q4;
#pragma unroll
    for (int c = 0; c < 8; ++c) vaddr[c] = (unsigned)(KT * DH * 2 + row * 256 + (((2 * c + (p4 >> 1)) ^ ((row & 7) << 1)) << 4) + 8 * (p4 & 1));
  }

  f32x4 s[2][3];
  bf16x8 kf[2][4];
  auto kread = [&](int slot) {
#pragma unroll
    for (int ks = 0; ks < 4; ++ks) {
      unsigned a = kaddr[ks] + (unsigned)(slot * STG_BYTES);
      asm volatile("ds_read_b128 %0, %1" : "=v"(kf[0][ks]) : "v"(a) : "memory");
      asm volatile("ds_read_b128 %0, %1 offset:4096" : "=v"(kf[1][ks]) : "v"(a) : "memory");
    }
  };
  auto kwait = [&]() {
    asm volatile("s_waitcnt lgkmcnt(0)" : "+v"(kf[0][0]), "+v"(kf[0][1]), "+v"(kf[0][2]), "+v"(kf[0][3]), "+v"(kf[1][0]), "+v"(kf[1][1]), "+v"(kf[1][2]), "+v"(kf[1][3]) :: "memory");
  };
  auto qk = [&]() {
#pragma unroll
    for (int j = 0; j < 3; ++j) { s[0][j] = f32x4{0.f, 0.f, 0.f, 0.f}; s[1][j] = f32x4{0.f, 0.f, 0.f, 0.f}; }
#pragma unroll
    for (int ks = 0; ks < 4; ++ks) {
#pragma unroll
      for (int j = 0; j < 3; ++j) {
        s[0][j] = __builtin_amdgcn_mfma_f32_16x16x32_bf16(kf[0][ks], qf[j][ks], s[0][j], 0, 0, 0);
        s[1][j] = __builtin_amdgcn_mfma_f32_16x16x32_bf16(kf[1][ks], qf[j][ks], s[1][j], 0, 0, 0);
      }
    }
  };
  asm volatile("s_waitcnt vmcnt(8)" ::: "memory");
  __builtin_amdgcn_s_barrier();
  kread(0); kwait(); qk();

  for (int it = 0; it < NIT; ++it) {
    if (it < NIT - 3) asm volatile("s_waitcnt vmcnt(4)" ::: "memory");
    else asm volatile("s_waitcnt vmcnt(0)" ::: "memory");
    __builtin_amdgcn_s_barrier();
    if (it + 3 < NIT) stage((it + 3) & 3, it + 3);
    bf16x4 vlo[8], vhi[8];
#pragma unroll
    for (int c = 0; c < 8; ++c) {
      unsigned a = vaddr[c] + (unsigned)((it & 3) * STG_BYTES);
      asm volatile("ds_read_b64_tr_b16 %0, %1" : "=v"(vlo[c]) : "v"(a) : "memory");
      asm volatile("ds_read_b64_tr_b16 %0, %1 offset:4096" : "=v"(vhi[c]) : "v"(a) : "memory");
    }
    float mx[3];
#pragma unroll
    for (int j = 0; j < 3; ++j) {
      float t = fmaxf(fmaxf(fmaxf(s[0][j][0], s[0][j][1]), fmaxf(s[0][j][2], s[0][j][3])),
                      fmaxf(fmaxf(s[1][j][0], s[1][j][1]), fmaxf(s[1][j][2], s[1][j][3])));
      auto r = __builtin_amdgcn_permlane32_swap(__float_as_uint(t), __float_as_uint(t), false, false);
      t = fmaxf(__uint_as_float(r[0]), __uint_as_float(r[1]));
      auto r2 = __builtin_amdgcn_permlane16_swap(__float_as_uint(t), __float_as_uint(t), false, false);
      t = fmaxf(__uint_as_float(r2[0]), __uint_as_float(r2[1]));
      mx[j] = t * cs;
    }
    if (!__all((mx[0] <= m[0] + THR) & (mx[1] <= m[1] + THR) & (mx[2] <= m[2] + THR))) {
#pragma unroll
      for (int j = 0; j < 3; ++j) {
        float mn = fmaxf(m[j], mx[j]);
        float alpha = __builtin_amdgcn_exp2f(m[j] - mn);
        m[j] = mn;
        l[j] *= alpha;
#pragma unroll
        for (int c = 0; c < 8; ++c) oacc[c][j] *= alpha;
      }
    }
    bf16x8 pb[3];
#pragma unroll
    for (int j = 0; j < 3; ++j) {
      float nm = -m[j];
      float p[8];
#pragma unroll
      for (int r = 0; r < 4; ++r) {
        p[r] = __builtin_amdgcn_exp2f(fmaf(s[0][j][r], cs, nm));
        p[4 + r] = __builtin_amdgcn_exp2f(fmaf(s[1][j][r], cs, nm));
      }
      l[j] += ((p[0] + p[1]) + (p[2] + p[3])) + ((p[4] + p[5]) + (p[6] + p[7]));
      u32x4 w = {cvtpk(p[0], p[1]), cvtpk(p[2], p[3]), cvtpk(p[4], p[5]), cvtpk(p[6], p[7])};
      pb[j] = *(bf16x8*)&w;
    }
    asm volatile("s_waitcnt lgkmcnt(0)" : "+v"(vlo[0]), "+v"(vlo[1]), "+v"(vlo[2]), "+v"(vlo[3]), "+v"(vlo[4]), "+v"(vlo[5]), "+v"(vlo[6]), "+v"(vlo[7]),
                 "+v"(vhi[0]), "+v"(vhi[1]), "+v"(vhi[2]), "+v"(vhi[3]), "+v"(vhi[4]), "+v"(vhi[5]), "+v"(vhi[6]), "+v"(vhi[7]) :: "memory");
#pragma unroll
    for (int c = 0; c < 8; ++c) {
      bf16x8 vf = {vlo[c][0], vlo[c][1], vlo[c][2], vlo[c][3], vhi[c][0], vhi[c][1], vhi[c][2], vhi[c][3]};
#pragma unroll
      for (int j = 0; j < 3; ++j) oacc[c][j] = __builtin_amdgcn_mfma_f32_16x16x32_bf16(vf, pb[j], oacc[c][j], 0, 0, 0);
    }
    if (it + 1 < NIT) { kread((it + 1) & 3); kwait(); qk(); }
  }
#pragma unroll
  for (int j = 0; j < 3; ++j) {
    float ls = l[j];
    auto r = __builtin_amdgcn_permlane32_swap(__float_as_uint(ls), __float_as_uint(ls), false, false);
    ls = __uint_as_float(r[0]) + __uint_as_float(r[1]);
    auto r2 = __builtin_amdgcn_permlane16_swap(__float_as_uint(ls), __float_as_uint(ls), false, false);
    ls = __uint_as_float(r2[0]) + __uint_as_float(r2[1]);
    float inv = 1.f / ls;
    int qrow = b * SQ + wid * 48 + j * 16 + fr;
    __hip_bfloat16* op = O + (size_t)br * MROWS * HD + (size_t)qrow * HD + h * DH + 4 * fg;
#pragma unroll
    for (int c = 0; c < 8; ++c) {
      u32x2 w = {cvtpk(oacc[c][j][0] * inv, oacc[c][j][1] * inv), cvtpk(oacc[c][j][2] * inv, oacc[c][j][3] * inv)};
      *(u32x2*)(op + c * 16) = w;
    }
  }
}

extern "C" void kernel_launch(void* const* d_in, const int* in_sizes, int n_in, void* d_out, int out_size, void* d_ws, size_t ws_size,
                              hipStream_t stream) {
  const __hip_bfloat16* A_x = (const __hip_bfloat16*)d_in[0];
  const __hip_bfloat16* A_k = (const __hip_bfloat16*)d_in[1];
  const __hip_bfloat16* A_v = (const __hip_bfloat16*)d_in[2];
  const __hip_bfloat16* A_wq = (const __hip_bfloat16*)d_in[3];
  const __hip_bfloat16* A_wo = (const __hip_bfloat16*)d_in[4];
  const __hip_bfloat16* B_x = (const __hip_bfloat16*)d_in[5];
  const __hip_bfloat16* B_k = (const __hip_bfloat16*)d_in[6];
  const __hip_bfloat16* B_v = (const __hip_bfloat16*)d_in[7];
  const __hip_bfloat16* B_wq = (const __hip_bfloat16*)d_in[8];
  const __hip_bfloat16* B_wo = (const __hip_bfloat16*)d_in[9];
  __hip_bfloat16* q = (__hip_bfloat16*)d_ws;
  __hip_bfloat16* o = q + (size_t)2 * MROWS * HD;
  __hip_bfloat16* out = (__hip_bfloat16*)d_out;

  (void)hipFuncSetAttribute((const void*)gemm_kernel<0>, hipFuncAttributeMaxDynamicSharedMemorySize, 98304);
  (void)hipFuncSetAttribute((const void*)gemm_kernel<1>, hipFuncAttributeMaxDynamicSharedMemorySize, 98304);
  (void)hipFuncSetAttribute((const void*)attn_kernel, hipFuncAttributeMaxDynamicSharedMemorySize, 65536);
  float* part = (float*)(o + (size_t)2 * MROWS * HD);
  gemm_kernel<0><<<2 * 12 * 32, 256, 98304, stream>>>(A_x, B_x, A_wq, B_wq, q, part);
  attn_kernel<<<512, 256, NSTG * STG_BYTES, stream>>>(A_k, A_v, B_k, B_v, q, o);
  gemm_kernel<1><<<2 * 12 * 4 * OSPLIT, 256, 98304, stream>>>(o, o + (size_t)MROWS * HD, A_wo, B_wo, out, part);
  oreduce_kernel<<<2 * MROWS * DM / 4 / 256, 256, 0, stream>>>(part, out);
}
```
